```python
import torch

def _softmax(x, axis=-1):
    return torch.softmax(x, dim=axis)
def _gelu(x):
    return torch.nn.functional.gelu(x, approximate="tanh")
def _silu(x):
    return torch.nn.functional.silu(x)
def _sigmoid(x):
    return torch.sigmoid(x)
def _layernorm(x, axis=-1, eps=1e-5):
    m = x.mean(dim=axis, keepdim=True)
    v = x.var(dim=axis, keepdim=True, unbiased=False)
    return (x - m) / torch.sqrt(v + eps)
def _scatter_add(tgt, idx, src, axis=0):
    return tgt.clone().index_add_(axis, idx, src)
def _segment_sum(src, seg, n):
    out = torch.zeros((n,) + src.shape[1:], dtype=src.dtype)
    return out.index_add_(0, seg, src)
def _pad(x, pw):
    flat = tuple(v for pair in reversed(pw) for v in pair)
    return torch.nn.functional.pad(x, flat)

kernel_name = 'sequential_preLN__gqa_block_cached__mixer_channel_mlp__decode'

def setup_inputs():

    _w = (lambda a: (lambda t: t.to(torch.bfloat16) if t.is_floating_point() else t)(torch.as_tensor(a)))
    def _A_setup():
        B, Sq, D = 4, 1, 512
        Hq, Hkv, Sk, Dh = 32, 8, 4096, 128
        assert Hq % Hkv == 0, (Hq, Hkv)
        return {
            "x": _w((lambda *s: torch.randn(*s, dtype=torch.bfloat16) * 0.05)(B, Sq, D)),
            "k_cache": _w((lambda *s: torch.randn(*s, dtype=torch.bfloat16) * 3.2)(B, Hkv, Sk, Dh)),
            "v_cache": _w((lambda *s: torch.randn(*s, dtype=torch.bfloat16) * 0.4)(B, Hkv, Sk, Dh)),
            "wq": _w((lambda *s: torch.randn(*s, dtype=torch.bfloat16) * 3.2)(D, Hq * Dh) * (D ** -0.5)),
            "wo": _w((lambda *s: torch.randn(*s, dtype=torch.bfloat16) * 0.4)(Hq * Dh, D) * ((Hq * Dh) ** -0.5)),
        }
    _A_in = _A_setup()

    def _B_setup():
        B, S, D, F = 4, 1, 512, 1792
        return {
            "x": _w((lambda *s: torch.randn(*s, dtype=torch.bfloat16) * 0.05)(B, S, D)),
            "w1": _w((lambda *s: torch.randn(*s, dtype=torch.bfloat16) * 3.2)(D, F) * (D ** -0.5)),
            "b1": _w((lambda *s: torch.randn(*s, dtype=torch.bfloat16) * 3.2)(F)),
            "w2": _w((lambda *s: torch.randn(*s, dtype=torch.bfloat16) * 0.4)(F, D) * (F ** -0.5)),
            "b2": _w((lambda *s: torch.randn(*s, dtype=torch.bfloat16) * 0.8)(D)),
        }
    _B_in = _B_setup()

    return {
        "A_x": _A_in["x"],
        "A_k_cache": _A_in["k_cache"],
        "A_v_cache": _A_in["v_cache"],
        "A_wq": _A_in["wq"],
        "A_wo": _A_in["wo"],
        "B_x": _B_in["x"],
        "B_w1": _B_in["w1"],
        "B_b1": _B_in["b1"],
        "B_w2": _B_in["w2"],
        "B_b2": _B_in["b2"],
    }

def reference(A_x, A_k_cache, A_v_cache, A_wq, A_wo, B_x, B_w1, B_b1, B_w2, B_b2):
    def _A_ref(x, k_cache, v_cache, wq, wo):
        B, Sq, D = x.shape
        Hq, Dh = 32, 128
        Hkv = k_cache.shape[1]
        rep = Hq // Hkv
        q = (lambda x, *d: x.permute(*d))((x @ wq).reshape(B, Sq, Hq, Dh), 0, 2, 1, 3)
        k = (lambda x, n, axis: torch.repeat_interleave(x, n, dim=axis))(k_cache, rep, axis=1)
        v = (lambda x, n, axis: torch.repeat_interleave(x, n, dim=axis))(v_cache, rep, axis=1)
        scores = torch.einsum("bhqd,bhkd->bhqk", q, k) * (Dh ** -0.5)
        p = _softmax(scores, axis=-1)
        o = torch.einsum("bhqk,bhkd->bhqd", p, v)
        return (lambda x, *d: x.permute(*d))(o, 0, 2, 1, 3).reshape(B, Sq, Hq * Dh) @ wo

    def _B_ref(x, w1, b1, w2, b2):
        y = _layernorm(x, axis=-1)
        h = _gelu(torch.einsum("bsd,df->bsf", y, w1) + b1)
        return torch.einsum("bsf,fd->bsd", h, w2) + b2

    x = A_x
    h = x + _A_ref(_layernorm(x, axis=-1), A_k_cache, A_v_cache, A_wq, A_wo)
    h2 = h + _B_ref(_layernorm(h, axis=-1), B_w1, B_b1, B_w2, B_b2)
    return h2
```

```cpp
#include <hip/hip_runtime.h>
#include <stdint.h>

namespace {
#define LAS __attribute__((address_space(3)))
typedef __bf16 bf16x2 __attribute__((ext_vector_type(2)));
typedef __bf16 bf16x8 __attribute__((ext_vector_type(8)));
typedef short s16x4 __attribute__((ext_vector_type(4)));
typedef float f32x2 __attribute__((ext_vector_type(2)));
typedef float f32x4 __attribute__((ext_vector_type(4)));
typedef unsigned v4u __attribute__((ext_vector_type(4)));
typedef unsigned v2u __attribute__((ext_vector_type(2)));
typedef __amdgpu_buffer_rsrc_t rsrc_t;

constexpr int SC1 = 16, NT = 2;
constexpr int DLY_Q = 40, DLY_M = 40, DLY_A = 5, DLY_D = 7, RETRY = 8;
constexpr int WS_EPOCH = 0, WS_Q = 4096, WS_ML = WS_Q + 262144, WS_OA = WS_ML + 32768, WS_HB = WS_OA + 262144, WS_HD = WS_HB + 32768,
              WS_END = WS_HD + 4 * 28 * 2048;
constexpr int L_O = 0, L_R0 = L_O + 8 * 2176, L_R1 = L_R0 + 8192, L_CS = L_R1 + 8192, L_H1 = L_CS + 2048, L_XR = L_H1 + 128,
              L_ML = L_XR + 512, L_MF = L_ML + 256, L_FLG = L_MF + 128, L_QF = L_FLG + 128, L_XN = L_QF + 1024, L_STG = L_XN + 1024, L_END = L_STG + 65536;
constexpr int F_QF = L_FLG, F_ML = L_FLG + 32;
constexpr float LOG2E = 1.4426950408889634f, SCALE = 0.08838834764831845f, EPS = 1e-5f;
constexpr unsigned SPIN_MAX = 1u << 18;
constexpr int OOB = 0x7ffffff0;

__device__ __forceinline__ rsrc_t mk(const void* p, unsigned bytes) { return __builtin_amdgcn_make_buffer_rsrc((void*)p, 0, (int)bytes, 0x00020000); }
template <int AUX> __device__ __forceinline__ v4u ld16(rsrc_t r, int vo, int so) { return __builtin_amdgcn_raw_buffer_load_b128(r, vo, so, AUX); }
template <int AUX> __device__ __forceinline__ void dma16(rsrc_t r, LAS unsigned char* dst, int vo, int so) { __builtin_amdgcn_raw_ptr_buffer_load_lds(r, (LAS void*)dst, 16, vo, so, 0, AUX); }
__device__ __forceinline__ unsigned ldu16(rsrc_t r, int vo, int so) { return (unsigned)__builtin_amdgcn_raw_buffer_load_b16(r, vo, so, 0); }
__device__ __forceinline__ unsigned pk(float a, float b) { f32x2 v = {a, b}; return __builtin_bit_cast(unsigned, __builtin_convertvector(v, bf16x2)); }
__device__ __forceinline__ float lo(unsigned u) { return __builtin_bit_cast(float, u << 16); }
__device__ __forceinline__ float hi(unsigned u) { return __builtin_bit_cast(float, u & 0xffff0000u); }
__device__ __forceinline__ float rbf(float a) { return lo(pk(a, a)); }
__device__ __forceinline__ float asf(unsigned u) { return __builtin_bit_cast(float, u); }
__device__ __forceinline__ unsigned asu(float f) { return __builtin_bit_cast(unsigned, f); }
template <int C> __device__ __forceinline__ float dpp(float x) { return __builtin_bit_cast(float, __builtin_amdgcn_mov_dpp(__builtin_bit_cast(int, x), C, 0xf, 0xf, true)); }
constexpr int XOR1 = 0xB1, XOR2 = 0x4E, HMIR = 0x141, ROR8 = 0x128, ROR4 = 0x124;
__device__ __forceinline__ float sw16add(float a, float b) { auto s = __builtin_amdgcn_permlane16_swap(asu(a), asu(b), false, false); return asf(s[0]) + asf(s[1]); }
__device__ __forceinline__ float sw32add(float a, float b) { auto s = __builtin_amdgcn_permlane32_swap(asu(a), asu(b), false, false); return asf(s[0]) + asf(s[1]); }
__device__ __forceinline__ float xsum(float x) { x = sw32add(x, x); return sw16add(x, x); }
__device__ __forceinline__ float rowsum(float x) { x += dpp<ROR8>(x); x += dpp<ROR4>(x); x += dpp<XOR2>(x); return x + dpp<XOR1>(x); }
__device__ __forceinline__ float xmax(float x) {
  auto s = __builtin_amdgcn_permlane32_swap(asu(x), asu(x), false, false);
  x = fmaxf(asf(s[0]), asf(s[1]));
  auto t = __builtin_amdgcn_permlane16_swap(asu(x), asu(x), false, false);
  return fmaxf(asf(t[0]), asf(t[1]));
}
__device__ __forceinline__ float max8(float x) { x = fmaxf(x, dpp<XOR1>(x)); x = fmaxf(x, dpp<XOR2>(x)); return fmaxf(x, dpp<HMIR>(x)); }
__device__ __forceinline__ float sum8(float x) { x += dpp<XOR1>(x); x += dpp<XOR2>(x); return x + dpp<HMIR>(x); }
__device__ __forceinline__ float dot2(unsigned a, unsigned b, float c) { return __builtin_amdgcn_fdot2_f32_bf16(__builtin_bit_cast(bf16x2, a), __builtin_bit_cast(bf16x2, b), c, false); }
__device__ __forceinline__ float ex2(float x) { return __builtin_amdgcn_exp2f(x); }
__device__ __forceinline__ f32x4 mfma(v4u a, v4u b, f32x4 c) {
  return __builtin_amdgcn_mfma_f32_16x16x32_bf16(__builtin_bit_cast(bf16x8, a), __builtin_bit_cast(bf16x8, b), c, 0, 0, 0);
}
__device__ __forceinline__ v2u trd(LAS unsigned char* p) { return __builtin_bit_cast(v2u, __builtin_amdgcn_ds_read_tr16_b64_v4i16((LAS s16x4*)p)); }
__device__ __forceinline__ v4u trfrag(LAS unsigned char* p0, LAS unsigned char* p1) { const v2u a = trd(p0), b = trd(p1); return v4u{a.x, a.y, b.x, b.y}; }
__device__ __forceinline__ float sum4(f32x4 v) { const f32x2 t = v.xy + v.zw; float r; asm("v_add_f32 %0, %1, %2" : "=v"(r) : "v"(t.x), "v"(t.y)); return r; }
#define PIN() __builtin_amdgcn_sched_barrier(0)
#define USE(r) asm volatile("" ::"v"(r))
#define KEEP(r) asm volatile("" : "+v"(r))
#define KEEPNV(r) asm("" : "+v"(r))
#define KEEPS(r) asm volatile("" : "+s"(r))
#define MEMB() asm volatile("" ::: "memory")
#define BAR() do { asm volatile("s_waitcnt lgkmcnt(0)" ::: "memory"); __builtin_amdgcn_s_barrier(); MEMB(); } while (0)
template <typename T> __device__ __forceinline__ T red8(LAS unsigned char* p, int stride) {
  T r[8];
#pragma unroll
  for (int i = 0; i < 8; ++i) r[i] = *(LAS T*)(p + i * stride);
  PIN();
  return ((r[0] + r[1]) + (r[2] + r[3])) + ((r[4] + r[5]) + (r[6] + r[7]));
}
__device__ __forceinline__ float fdiv(float x, float d, float r) { const float q = x * r; return fmaf(fmaf(-q, d, x), r, q); }
__device__ __forceinline__ bool tg(v4u a, unsigned tag) { return (a.y == tag) & (a.w == tag); }
template <int SLP> __device__ __forceinline__ v4u poll1(rsrc_t ws, int vo, int so, unsigned tag) {
  v4u a;
  for (unsigned spin = 0;; ++spin) {
    MEMB();
    a = ld16<SC1>(ws, vo, so);
    if (__all(tg(a, tag)) || spin > SPIN_MAX) break;
    if (SLP) __builtin_amdgcn_s_sleep(SLP);
  }
  return a;
}
__device__ __forceinline__ void st4(unsigned v, unsigned mask, unsigned bits, rsrc_t r, int vo, int so) { __builtin_amdgcn_raw_buffer_store_b32((v & ~mask) | bits, r, vo, so, SC1); }
__device__ __forceinline__ bool clean(v4u x, unsigned mask) { return (((x.x | x.y) | (x.z | x.w)) & mask) == 0u; }
__device__ __forceinline__ v4u pollw(rsrc_t ws, int vo, int so, v4u bits, unsigned mask) {
  v4u a;
  for (unsigned spin = 0;; ++spin) {
    MEMB();
    a = ld16<SC1>(ws, vo, so) ^ bits;
    if (__builtin_expect(__all(clean(a, mask)), 1)) break;
    asm volatile("" : "+s"(spin));
    if (spin > SPIN_MAX) break;
  }
  return a;
}
constexpr unsigned MQ = 3u, MF = 0xFFu, MB = 0x00010001u;
constexpr float RND7 = 1.f + 1.f / 512.f;
__device__ __forceinline__ unsigned mktag(unsigned e) {
  unsigned t = (e & 0xFFu) % 251u + 1u;
  t += t >= 0x55u; t += t >= 0xAAu;
  return ((e * 0x9E3779B1u + 0x7F4A7C15u) & ~0xFFu) | t;
}
__device__ __forceinline__ unsigned bbits(unsigned tag, int i) { return ((tag >> (2 * i)) & 1u) | (((tag >> (2 * i + 1)) & 1u) << 16); }
__device__ __forceinline__ float add(float a, float b) { float r; asm("v_add_f32 %0, %1, %2" : "=v"(r) : "v"(a), "v"(b)); return r; }
__device__ __forceinline__ float pick(f32x4 e, f32x4 o) { const f32x2 a = e.xy + e.zw, b = o.xy + o.zw; return add(a.x, b.y); }
__device__ __forceinline__ float sel4(int lane, float a, float b, float c, float d) { const float l = (lane & 16) ? b : a, h = (lane & 16) ? d : c; return (lane & 32) ? h : l; }
__device__ __forceinline__ void flag_set(LAS unsigned char* f, unsigned tag) { MEMB(); *(volatile LAS unsigned*)f = tag; MEMB(); }
__device__ __forceinline__ void flag_wait(LAS unsigned char* f, unsigned tag) {
  for (unsigned spin = 0; spin < (SPIN_MAX << 6); ++spin) {
    MEMB();
    const unsigned v = *(volatile LAS unsigned*)f;
    MEMB();
    if (__all(v == tag)) break;
    __builtin_amdgcn_s_sleep(1);
  }
}

__global__ void __launch_bounds__(576, 3)
fused_decode(const unsigned char* __restrict__ kp, const unsigned char* __restrict__ wqp, const unsigned char* __restrict__ xp, unsigned char* wsp, const unsigned char* __restrict__ vp,
             const void* __restrict__ wop, const void* __restrict__ w1p, const void* __restrict__ w2p, const void* __restrict__ b1p, const void* __restrict__ b2p, void* outp) {
  __shared__ __attribute__((aligned(16))) unsigned char smem[L_END];
  LAS unsigned char* const L = (LAS unsigned char*)smem;
  const int lane = threadIdx.x & 63, w = __builtin_amdgcn_readfirstlane(threadIdx.x >> 6);
  const int bid = blockIdx.x, g = bid & 7, j = bid >> 3, b = j >> 3, s = j & 7;
  const int gq = lane >> 4, n = lane & 15, tq = (lane >> 2) & 3, tp = lane & 3, vo = lane * 16;
  const unsigned slab = (unsigned)((b * 8 + g) * 8 + s) * 131072u;
  const int ko = tq * 256 + gq * 64 + tp * 16;
  asm volatile("" ::"s"(kp), "s"(wqp), "s"(xp), "s"(wsp), "s"(vp), "s"(wop), "s"(w1p), "s"(w2p), "s"(b1p), "s"(b2p), "s"(outp), "v"(ko), "s"(slab));
  const rsrc_t rk = mk(kp + slab, w < 8 ? 131072 : 0);
  const v4u f2 = ld16<NT>(rk, ko, w * 4096), f3 = ld16<NT>(rk, ko + 1024, w * 4096), f4 = ld16<NT>(rk, ko + 2048, w * 4096);
  PIN();
  const rsrc_t rws = mk(wsp, WS_END);
  const bool isw = (b & 1) == 0;
  const int mb = s & 3, ls = (b >> 1) * 2 + (s >> 2);
  const bool ism = !isw && ls < (g < 4 ? 4 : 3);
  const int fc = ism ? 3 * g + (g < 4 ? g : 4) + ls : 0;
  const f32x4 z4 = {0.f, 0.f, 0.f, 0.f};

#define FRAGS(tile, dst) do { \
    const int rsw = (((tq >> 1) & 1) | ((gq & 1) << 1)); \
    _Pragma("unroll") for (int h = 0; h < 2; ++h) _Pragma("unroll") for (int t = 0; t < 4; ++t) { \
      LAS unsigned char* a = (tile) + h * 4096 + gq * 1024 + tq * 128 + ((t ^ rsw) * 32) + tp * 8; dst[h][t] = trfrag(a, a + 512); } } while (0)
#define STAGE64(stg, src, dst) do { \
    _Pragma("unroll") for (int i = 0; i < 8; ++i) *(LAS v4u*)(stg + i * 1024 + (lane >> 3) * 128 + (((lane & 7) * 16) ^ ((((lane >> 4) & 1) | ((i & 1) << 1)) << 5))) = src[i]; \
    FRAGS(stg, dst); } while (0)

  if (__builtin_expect(w < 8, 0)) {
  const rsrc_t rv = mk(vp + slab, 131072);
  const rsrc_t rw2 = mk(w2p, ism ? 1792 * 512 * 2 : 0);
  const rsrc_t rwx = mk(isw ? wop : w1p, isw ? 4096 * 512 * 2 : (ism ? 512 * 1792 * 2 : 0));
  const int xstr = isw ? 1024 : 3584, xbase = isw ? (g * 512 + w * 64) * 1024 + s * 128 : w * 64 * 3584 + fc * 128;
  LAS unsigned char* const stg = L + L_STG + w * 8192;
  const v4u zero4 = {0u, 0u, 0u, 0u}, ones = {0x3F803F80u, 0x3F803F80u, 0x3F803F80u, 0x3F803F80u};
  v4u K[16], V[16], wov[8], w2v[8], Q, P[2];
  f32x4 acc[2] = {z4, z4};
  float sc[16], mx;
  unsigned tag;
  const int wro = (lane >> 3) * 1024 + (lane & 7) * 16, xro = (lane >> 3) * xstr + (lane & 7) * 16;
#define LDK(m) K[m] = ld16<NT>(rk, ko + ((m) & 3) * 1024, w * 4096 + ((m) >> 2) * 32768)
#define LDV(m) V[m] = ld16<NT>(rv, vo + ((m) & 3) * 1024, w * 4096 + ((m) >> 2) * 32768)
#define LDO(m) wov[m] = ld16<0>(rwx, xro, xbase + (m) * 8 * xstr)
#define QKPART(c) do { \
    f32x4 d[4]; \
    _Pragma("unroll") for (int m = 0; m < 4; ++m) d[m] = mfma(K[4 * (c) + m], Q, z4); \
    PIN(); \
    _Pragma("unroll") for (int m = 0; m < 4; ++m) { sc[4 * (c) + m] = dpp<0xFF>(d[m].w) + (dpp<0xAA>(d[m].z) + (dpp<0x55>(d[m].y) + dpp<0x00>(d[m].x))); KEEPNV(sc[4 * (c) + m]); } \
    _Pragma("unroll") for (int e = 2 * (c); e < 2 * (c) + 2; ++e) { const unsigned u = pk(sc[2 * e], sc[2 * e + 1]); const unsigned t = pk(lo(u) * SCALE, hi(u) * SCALE); sc[2 * e] = lo(t); sc[2 * e + 1] = hi(t); } \
    PIN(); } while (0)
#define SOFTMAX_A() do { mx = sc[0]; \
    _Pragma("unroll") for (int m = 1; m < 16; ++m) mx = fmaxf(mx, sc[m]); \
    mx = xmax(mx); \
    const float nm = -mx * LOG2E; \
    _Pragma("unroll") for (int m = 0; m < 16; ++m) sc[m] = ex2(fmaf(sc[m], LOG2E, nm)); PIN(); } while (0)
#define SOFTMAX_B() do { float l0 = 0.f, l1 = 0.f; \
    _Pragma("unroll") for (int c = 0; c < 8; ++c) { l0 += sc[2 * c]; l1 += sc[2 * c + 1]; P[c >> 2][c & 3] = pk(sc[2 * c], sc[2 * c + 1]); } \
    const float l = xsum(l0 + l1); \
    if ((lane & 0x33) == 0) *(LAS f32x2*)(L + L_ML + tq * 64 + w * 8) = f32x2{mx, l}; \
    flag_set(L + F_ML + w * 4, tag); \
    PIN(); } while (0)
#define PVSTEP(st_) do { \
    _Pragma("unroll") for (int t = 0; t < 4; ++t) { const v4u Bt = (tp == t) ? P[st_] : zero4; \
      _Pragma("unroll") for (int e = 0; e < 2; ++e) { v4u A; \
        _Pragma("unroll") for (int i = 0; i < 4; ++i) A[i] = __builtin_amdgcn_perm(V[8 * (st_) + 2 * i + 1][t], V[8 * (st_) + 2 * i][t], e ? 0x07060302u : 0x05040100u); \
        acc[e] = mfma(A, Bt, acc[e]); } } PIN(); } while (0)
  v4u PA[8], PB[4];
#define PVPRE() do { \
    _Pragma("unroll") for (int t = 0; t < 4; ++t) { PB[t] = (tp == t) ? P[1] : zero4; KEEP(PB[t]); \
      _Pragma("unroll") for (int e = 0; e < 2; ++e) _Pragma("unroll") for (int i = 0; i < 3; ++i) { \
        PA[2 * t + e][i] = __builtin_amdgcn_perm(V[9 + 2 * i][t], V[8 + 2 * i][t], e ? 0x07060302u : 0x05040100u); KEEP(PA[2 * t + e][i]); } } \
    PIN(); } while (0)
#define PACE(done, ld) do { USE(done); ld; PIN(); } while (0)
  K[0] = f2; K[1] = f3; K[2] = f4;
  PIN();
  PACE(K[0], LDK(3));   PACE(K[1], LDK(4));   PACE(K[2], LDK(5));   PACE(K[3], LDK(6));   PACE(K[4], LDK(7));   PACE(K[5], LDK(8));
  PACE(K[6], LDK(9));   PACE(K[7], LDK(10));  PACE(K[8], LDK(11));  PACE(K[9], LDK(12));  PACE(K[10], LDK(13)); PACE(K[11], LDK(14));
  PACE(K[12], LDK(15)); PACE(K[13], LDV(0));  PACE(K[14], LDV(1));  PACE(K[15], LDV(2));  PACE(V[0], LDV(3));   PACE(V[1], LDV(4));
  BAR();
  tag = __builtin_amdgcn_readfirstlane(*(LAS unsigned*)(L + F_QF));
  Q = *(LAS v4u*)(L + L_QF + tq * 256 + gq * 64 + tp * 16);
  QKPART(0); LDV(5); PIN();
  QKPART(1); LDV(6); PIN();
  QKPART(2); LDV(7); PIN();
  QKPART(3); SOFTMAX_A(); LDV(8); PIN();
  SOFTMAX_B();
  USE(V[2]); USE(V[3]); USE(V[4]); USE(V[5]);
  PACE(V[6], LDV(9));   PACE(V[7], LDV(10));
  PVSTEP(0);
  PACE(V[8], LDV(11));  PACE(V[9], LDV(12));  PACE(V[10], LDV(13)); PACE(V[11], LDV(14)); PACE(V[12], LDV(15));
  LDO(0); LDO(1); LDO(2);
  USE(V[13]); PIN();
  LDO(3); LDO(4); LDO(5);
  PIN(); PVPRE();
  USE(V[14]); PIN();
  LDO(6); LDO(7);
  PIN();
  const v4u tb = {bbits(tag, 0), bbits(tag, 1), bbits(tag, 2), bbits(tag, 3)};
  unsigned tbw = bbits(tag, tp);
  int owr = L_O + w * 2176 + lane * 8, ord = L_O + ((lane >> 3) & 3) * 544 + ((lane >> 5) * 32 + ((lane >> 2) & 1) * 16 + w * 4 + tp) * 8, omf = L_MF + w * 32, l4 = lane * 4;
  int oas = WS_OA + ((b * 8 + g) * 8 + s) * 1024 + w * 256;
  KEEP(tbw); KEEP(owr); KEEP(ord); KEEP(omf); KEEP(l4); KEEPS(oas);
  asm volatile("" ::"s"(tb.x), "s"(tb.y), "s"(tb.z), "s"(tb.w));
  PIN();
#pragma unroll
  for (int jj = 0; jj < 8; ++jj) PA[jj][3] = __builtin_amdgcn_perm(V[15][jj >> 1], V[14][jj >> 1], (jj & 1) ? 0x07060302u : 0x05040100u);
#pragma unroll
  for (int jj = 0; jj < 8; ++jj) acc[jj & 1] = mfma(PA[jj], PB[jj >> 1], acc[jj & 1]);
#pragma unroll
  for (int r = 0; r < 4; ++r) *(LAS f32x2*)(L + owr + r * 544) = f32x2{acc[0][r], acc[1][r]};
  PIN();
  BAR();
  if (w < 4) {
    f32x2 r[8];
    const f32x4 f0 = *(LAS f32x4*)(L + omf), f1 = *(LAS f32x4*)(L + omf + 16);
#pragma unroll
    for (int i = 0; i < 8; ++i) r[i] = *(LAS f32x2*)(L + ord + i * 2176);
    PIN();
    const f32x2 o = ((r[0] * f0.x + r[1] * f0.y) + (r[2] * f0.z + r[3] * f0.w)) + ((r[4] * f1.x + r[5] * f1.y) + (r[6] * f1.z + r[7] * f1.w));
    st4(pk(o.x, o.y), MB, tbw, rws, l4, oas);
  }
  PIN();
  if (isw) {
    const int ao = (n >> 1) * 1024 + gq * 32 + (lane & 1) * 16, as = WS_OA + (b * 8 + g) * 8192 + w * 128;
    v4u Bw[2][4];
    __builtin_amdgcn_s_sleep(DLY_A);
    v4u A0 = ld16<SC1>(rws, ao, as), A1 = ld16<SC1>(rws, ao, as + 65536);
    PIN();
    STAGE64(stg, wov, Bw);
    int rwr = L_R0 + w * 1024 + vo, rrd = L_R0 + (w & 1) * 8192 + vo, rxr = L_XR + (w & 1) * 256 + (lane >> 5) * 128 + n * 8;
    int n4 = (lane & 16) ? OOB : (lane >> 5) * 64 + n * 4;
    int hbs = WS_HB + ((b + (w & 1)) * 8 + s) * 1024 + g * 128;
    KEEP(rwr); KEEP(rrd); KEEP(rxr); KEEP(n4); KEEPS(hbs);
    A0 ^= tb; A1 ^= tb;
    if (!__all(clean(A0 | A1, MB))) {
      for (unsigned spin = 0;; ++spin) {
        MEMB();
        A0 = ld16<SC1>(rws, ao, as) ^ tb; A1 = ld16<SC1>(rws, ao, as + 65536) ^ tb;
        if (__builtin_expect(__all(clean(A0 | A1, MB)), 1)) break;
        asm volatile("" : "+s"(spin));
        if (spin > SPIN_MAX) break;
      }
    }
    v4u X, Y;
#pragma unroll
    for (int i = 0; i < 4; ++i) {
      const unsigned n1 = __builtin_amdgcn_mov_dpp(A1[i], XOR1, 0xf, 0xf, true), n0 = __builtin_amdgcn_mov_dpp(A0[i], XOR1, 0xf, 0xf, true);
      X[i] = (lane & 1) ? n1 : A0[i]; Y[i] = (lane & 1) ? A1[i] : n0;
    }
    f32x4 d[4];
#pragma unroll
    for (int t = 0; t < 4; ++t) d[t] = mfma(X, Bw[0][t], z4);
#pragma unroll
    for (int t = 0; t < 4; ++t) d[t] = mfma(Y, Bw[1][t], d[t]);
    PIN();
    const f32x2 u0 = d[0].xy + d[0].zw, u1 = d[1].xy + d[1].zw, u2 = d[2].xy + d[2].zw, u3 = d[3].xy + d[3].zw;
    *(LAS f32x4*)(L + rwr) = f32x4{u0.x, u1.x, u2.x, u3.x};
    *(LAS f32x4*)(L + rwr + 8192) = f32x4{u0.y, u1.y, u2.y, u3.y};
    BAR();
    if (w < 2) {
      const f32x2 xr = *(LAS f32x2*)(L + rxr);
      const f32x4 t = red8<f32x4>(L + rrd, 1024);
      float a = sw32add(t.x, t.z), c = sw32add(t.y, t.w);
      a = sw16add(a, a); c = sw16add(c, c);
      st4(pk(fmaf(a, RND7, xr.x), fmaf(c, RND7, xr.y)), MB, tbw, rws, n4, hbs);
    }
#pragma unroll
    for (int i = 0; i < 8; ++i) asm volatile("" : "=v"(wov[i]), "=v"(w2v[i]));
  }
  if (ism) {
    v4u B1[2][4], B2[2][4];
#pragma unroll
    for (int i = 0; i < 8; ++i) w2v[i] = ld16<0>(rw2, wro, (fc * 64 + i * 8) * 1024 + w * 128);
    PIN();
    const unsigned b2u = ldu16(mk(b2p, 1024), (lane & 1) * 8 + gq * 2, w * 128 + (fc & 7) * 16);
    PIN();
#pragma unroll
    for (int i = 0; i < 8; ++i) *(LAS v4u*)(stg + i * 1024 + (lane >> 3) * 128 + (((lane & 7) * 16) ^ ((i & 3) << 5))) = wov[i];
#pragma unroll
    for (int h = 0; h < 2; ++h)
#pragma unroll
      for (int t = 0; t < 4; ++t) {
        LAS unsigned char* a = stg + (gq >> 1) * 4096 + (tq & 1) * 2048 + (gq & 1) * 1024 + h * 512 + (tq >> 1) * 128 + ((t ^ ((gq & 1) | ((tq & 1) << 1))) * 32) + tp * 8;
        B1[h][t] = trfrag(a, a + 256);
      }
    {
      f32x4 c[4];
#pragma unroll
      for (int t = 0; t < 4; ++t) c[t] = mfma(ones, B1[1][t], mfma(ones, B1[0][t], z4));
      *(LAS float*)(L + L_CS + w * 256 + lane * 4) = sel4(lane, c[0].x, c[1].x, c[2].x, c[3].x);
    }
    BAR();
    const int bo0 = (n >> 1) * 128 + gq * 32 + (lane & 1) * 16;
    USE(b2u);
    v4u A = ld16<SC1>(rws, bo0, WS_HB + (mb * 8 + w) * 1024);
    PIN();
    STAGE64(stg, w2v, B2);
    int bo = (n >> 1) * 128 + gq * 32 + (lane & 1) * 16, r0 = L_R0 + w * 1024 + vo, r1 = L_R1 + w * 512 + lane * 8, hr = L_H1 + gq * 16;
    int hds = WS_HD + (mb * 28 + fc) * 2048 + w * 256;
    v4u one4 = ones;
    KEEP(bo); KEEP(r0); KEEP(r1); KEEP(hr); KEEPS(hds); KEEP(one4);
    A ^= tb;
    if (!__all(clean(A, MB))) A = pollw(rws, bo, WS_HB + (mb * 8 + w) * 1024, tb, MB);
    const f32x4 dg = mfma(A, A, z4), ds = mfma(A, one4, z4);
    f32x4 d[4][2], u;
#pragma unroll
    for (int t = 0; t < 4; ++t) { d[t][0] = mfma(A, B1[0][t], z4); d[t][1] = mfma(A, B1[1][t], z4); }
    PIN();
    const f32x2 dgp = dg.xy + dg.zw;
    *(LAS f32x2*)(L + r1) = f32x2{(lane & 1) ? dgp.y : dgp.x, sum4(ds)};
#pragma unroll
    for (int t = 0; t < 4; ++t) u[t] = pick(d[t][0], d[t][1]);
    *(LAS f32x4*)(L + r0) = u;
    BAR();
    BAR();
    const v4u H0 = *(LAS v4u*)(L + hr), H1 = *(LAS v4u*)(L + hr + 64);
    f32x4 e[4];
#pragma unroll
    for (int t = 0; t < 4; ++t) e[t] = mfma(H0, B2[0][t], z4);
#pragma unroll
    for (int t = 0; t < 4; ++t) e[t] = mfma(H1, B2[1][t], e[t]);
    st4(asu(sel4(lane, e[0].x, e[1].x, e[2].x, e[3].x)), MF, tag & 0xFFu, rws, l4, hds);
    if (fc < 8) {
      float hres;
      {
        const int src = (((fc >> 2) & 1) * 32 + (fc & 1) * 16 + n) * 4;
        const unsigned hx = __builtin_amdgcn_ds_bpermute(src, A.x), hy = __builtin_amdgcn_ds_bpermute(src, A.y), hz = __builtin_amdgcn_ds_bpermute(src, A.z), hw = __builtin_amdgcn_ds_bpermute(src, A.w);
        const unsigned hl = (lane & 16) ? hy : hx, hh = (lane & 16) ? hw : hz, hs = (lane & 32) ? hh : hl;
        float h = (fc & 2) ? hi(hs) : lo(hs);
        h += dpp<ROR8>(h); h += dpp<ROR4>(h); h += dpp<XOR2>(h);
        hres = rbf(h);
      }
      int fo = lane < 56 ? (lane >> 1) * 2048 + (lane & 1) * 16 : OOB, oo = (lane & 14) ? OOB : (lane & 1) * 8 + gq * 2;
      KEEP(hres); KEEP(fo); KEEP(oo);
      unsigned t8 = lane < 56 ? tag & 0xFFu : 0u;
      float b2v = lo(b2u);
      int os = mb * 1024 + w * 128 + fc * 16;
      KEEP(t8); KEEP(b2v); KEEPS(os);
      const v4u tf = {t8, t8, t8, t8};
      __builtin_amdgcn_s_sleep(DLY_D);
      const v4u a = pollw(rws, fo, WS_HD + mb * 28 * 2048 + w * 256 + fc * 32, tf, MF);
      float t = sw16add(sw32add(asf(a.x), asf(a.z)), sw32add(asf(a.y), asf(a.w)));
      t += dpp<ROR8>(t); t += dpp<ROR4>(t); t += dpp<XOR2>(t);
      const float res = hres + rbf(rbf(t) + b2v);
      __builtin_amdgcn_raw_buffer_store_b16((unsigned short)pk(res, res), mk(outp, 4096), oo, os, 0);
    }
  }
  } else {
    __builtin_amdgcn_s_setprio(3);
    const rsrc_t rq = mk(wqp, 512 * 4096 * 2), rx = mk(xp, 4096);
    v4u xs[4];
#pragma unroll
    for (int r = 0; r < 4; ++r) xs[r] = ld16<0>(rx, vo + r * 1024, 0);
    const unsigned epoch = __builtin_amdgcn_raw_buffer_load_b32(rws, 0, WS_EPOCH, SC1);
    PIN();
#pragma unroll
    for (int i = 0; i < 16; ++i)
      dma16<NT>(rq, L + L_STG + i * 1024, (lane >> 3) * 8192 + (((lane & 7) * 16) ^ (((gq & 1) | ((i & 1) << 1)) << 5)), ((j & 3) * 128 + i * 8) * 8192 + g * 1024 + (j >> 2) * 128);
    PIN();
    const unsigned preq = __builtin_amdgcn_raw_buffer_load_b32(rws, lane < 8 ? WS_Q + ((lane >> 1) * 8 + g) * 8192 + (j & 3) * 2048 + (j >> 2) * 256 + (lane & 1) * 128 : lane == 8 ? WS_ML + ((b * 8 + g) * 8 + s) * 128 : OOB, 0, SC1);
    PIN();
    const rsrc_t rb1 = mk(b1p, 3584);
    {
      float s1[4], s2[4];
#pragma unroll
      for (int r = 0; r < 4; ++r) {
        *(LAS v4u*)(L + L_O + r * 1024 + vo) = xs[r];
        s1[r] = 0.f; s2[r] = 0.f;
#pragma unroll
        for (int c = 0; c < 4; ++c) { s1[r] = dot2(xs[r][c], 0x3F803F80u, s1[r]); s2[r] = dot2(xs[r][c], xs[r][c], s2[r]); }
      }
      const v4u xf = *(LAS v4u*)(L + L_O + gq * 1024 + (j & 3) * 256 + n * 16);
      const float mean = rowsum(sw16add(sw32add(s1[0], s1[2]), sw32add(s1[1], s1[3]))) * (1.f / 512.f);
      const float var = fmaxf(fmaf(-mean, mean, rowsum(sw16add(sw32add(s2[0], s2[2]), sw32add(s2[1], s2[3]))) * (1.f / 512.f)), 0.f);
      const float mb = rbf(mean), den = rbf(__builtin_amdgcn_sqrtf(rbf(rbf(var) + EPS))), rden = __builtin_amdgcn_rcpf(den);
      v4u xn;
#pragma unroll
      for (int c = 0; c < 4; ++c) { const unsigned d = pk(lo(xf[c]) - mb, hi(xf[c]) - mb); xn[c] = pk(fdiv(lo(d), den, rden), fdiv(hi(d), den, rden)); }
      *(LAS v4u*)(L + L_XN + vo) = xn;
    }
    const unsigned tag = mktag(__builtin_amdgcn_readfirstlane(epoch));
    const v4u tf = {tag & 3u, (tag >> 2) & 3u, (tag >> 4) & 3u, (tag >> 6) & 3u};
    {
      f32x4 d[4] = {z4, z4, z4, z4};
#pragma unroll
      for (int c = 0; c < 2; ++c) {
        v4u Bq[2][4];
        asm volatile("s_waitcnt vmcnt(0)" ::: "memory");
        FRAGS(L + L_STG + c * 8192, Bq);
#pragma unroll
        for (int h = 0; h < 2; ++h) {
          const v4u xa = *(LAS v4u*)(L + L_XN + tp * 256 + c * 128 + h * 64 + gq * 16);
#pragma unroll
          for (int t = 0; t < 4; ++t) d[t] = mfma(xa, Bq[h][t], d[t]);
        }
      }
      USE(preq);
      const f32x4 dl = (lane & 16) ? d[1] : d[0], dh = (lane & 16) ? d[3] : d[2], q = (lane & 32) ? dh : dl;
#pragma unroll
      for (int r = 0; r < 4; ++r) st4(asu(q[r]), MQ, (tag >> (2 * tp)) & 3u, rws, lane * 4, WS_Q + (r * 8 + g) * 8192 + (j & 3) * 2048 + (j >> 2) * 256);
    }
    const unsigned xr0u = ldu16(rx, lane * 2, (b & 2) * 1024 + s * 128), xr1u = ldu16(rx, lane * 2, (b & 2) * 1024 + 1024 + s * 128);
    const unsigned b1u = ldu16(rb1, lane * 2, fc * 128);
    __builtin_amdgcn_s_sleep(DLY_Q);
    {
      v4u a[8];
      for (unsigned spin = 0;; ++spin) {
        MEMB();
#pragma unroll
        for (int i = 0; i < 8; ++i) a[i] = ld16<SC1>(rws, vo + (i & 1) * 1024, WS_Q + (b * 8 + g) * 8192 + (i >> 1) * 2048);
        PIN();
#pragma unroll
        for (int i = 0; i < 8; ++i) a[i] ^= tf;
        if (__all(clean(((a[0] | a[1]) | (a[2] | a[3])) | ((a[4] | a[5]) | (a[6] | a[7])), MQ)) || spin > SPIN_MAX) break;
        __builtin_amdgcn_s_sleep(RETRY);
      }
#pragma unroll
      for (int h = 0; h < 2; ++h) {
        const f32x4 q = (__builtin_bit_cast(f32x4, a[h]) + __builtin_bit_cast(f32x4, a[2 + h])) + (__builtin_bit_cast(f32x4, a[4 + h]) + __builtin_bit_cast(f32x4, a[6 + h]));
        *(LAS v2u*)(L + L_QF + h * 512 + lane * 8) = v2u{pk(q.x, q.y), pk(q.z, q.w)};
      }
      flag_set(L + F_QF, tag);
      BAR();
    }
    flag_wait(L + F_ML + (lane & 7) * 4, tag);
    {
      const f32x2 e = *(LAS f32x2*)(L + L_ML + (lane & 31) * 8);
      const float M = max8(e.x), Ls = sum8(e.y * ex2((e.x - M) * LOG2E));
      if ((lane & 0x27) == 0) __builtin_amdgcn_raw_buffer_store_b128(v4u{asu(M), tag, asu(Ls), tag}, rws, (lane >> 3) * 16, WS_ML + ((b * 8 + g) * 8 + s) * 128, SC1);
      const unsigned pre = __builtin_amdgcn_raw_buffer_load_b32(rws, lane < 8 ? WS_OA + ((b * 8 + g) * 8 + s) * 1024 + lane * 128 : lane == 8 ? WS_HB + bid * 128 : lane < 16 ? WS_HD + (bid * 7 + lane - 9) * 128 : OOB, 0, SC1);
      __builtin_amdgcn_s_sleep(DLY_M);
      const v4u ml = poll1<RETRY>(rws, (lane & 7) * 128 + ((lane >> 3) & 3) * 16, WS_ML + (b * 8 + g) * 1024, tag);
      USE(pre);
      const float Mg = max8(asf(ml.x)), Lg = sum8(asf(ml.z) * ex2((asf(ml.x) - Mg) * LOG2E));
      *(LAS float*)(L + L_MF + (lane & 31) * 4) = ex2((e.x - Mg) * LOG2E) * (RND7 * __builtin_amdgcn_rcpf(Lg));
    }
    *(LAS float*)(L + L_XR + (lane >> 5) * 128 + n * 8 + (gq & 1) * 4) = g == 0 ? lo(xr0u) * RND7 : 0.f;
    *(LAS float*)(L + L_XR + 256 + (lane >> 5) * 128 + n * 8 + (gq & 1) * 4) = g == 0 ? lo(xr1u) * RND7 : 0.f;
    float b1v = lo(b1u);
    KEEP(b1v);
    BAR();
    if (isw) BAR();
    if (ism) {
      BAR();
      float cs = red8<float>(L + L_CS + lane * 4, 256);
      int h1o = L_H1 + lane * 2;
      KEEP(cs); KEEP(h1o);
      BAR();
      f32x2 mr[8];
      f32x4 tr[8];
#pragma unroll
      for (int i = 0; i < 8; ++i) mr[i] = *(LAS f32x2*)(L + L_R1 + lane * 8 + i * 512);
#pragma unroll
      for (int i = 0; i < 8; ++i) tr[i] = *(LAS f32x4*)(L + L_R0 + vo + i * 1024);
      PIN();
      const f32x2 m = ((mr[0] + mr[1]) + (mr[2] + mr[3])) + ((mr[4] + mr[5]) + (mr[6] + mr[7]));
      const f32x4 t = ((tr[0] + tr[1]) + (tr[2] + tr[3])) + ((tr[4] + tr[5]) + (tr[6] + tr[7]));
      const float z = sw16add(sw32add(t.x, t.z), sw32add(t.y, t.w));
      float st = sw32add(m.x, m.y);
      st = rowsum(sw16add(st, st));
      const auto sb = __builtin_amdgcn_permlane32_swap(asu(st), asu(st), false, false);
      const float mu = asf(sb[1]) * (1.f / 8192.f), var = fmaxf(fmaf(-mu, mu, asf(sb[0]) * (1.f / 512.f)), 0.f);
      const float y = fmaf(fmaf(-mu, cs, z), __builtin_amdgcn_rsqf(fmaf(var, 1.f + EPS, EPS * EPS)), b1v);
      constexpr float C0 = -2.f * LOG2E * 0.7978845608028654f, C1 = C0 * 0.044715f;
      const float gl = y * __builtin_amdgcn_rcpf(1.f + ex2(y * fmaf(y * y, C1, C0)));
      *(LAS unsigned short*)(L + h1o) = (unsigned short)pk(gl, gl);
      BAR();
      if (bid == 8 * 8 && lane == 0) __builtin_amdgcn_raw_buffer_store_b32(tag, rws, 0, WS_EPOCH, SC1);
    }
  }

}
}

extern "C" void kernel_launch(void* const* d_in, const int* in_sizes, int n_in, void* d_out, int out_size, void* d_ws, size_t ws_size, hipStream_t stream) {
  hipLaunchKernelGGL(fused_decode, dim3(256), dim3(576), 0, stream, (const unsigned char*)d_in[1], (const unsigned char*)d_in[3], (const unsigned char*)d_in[0], (unsigned char*)d_ws,
                     (const unsigned char*)d_in[2], d_in[4], d_in[6], d_in[8], d_in[7], d_in[9], d_out);
}
```
